```python
import torch

def _softmax(x, axis=-1):
    return torch.softmax(x, dim=axis)
def _gelu(x):
    return torch.nn.functional.gelu(x, approximate="tanh")
def _silu(x):
    return torch.nn.functional.silu(x)
def _sigmoid(x):
    return torch.sigmoid(x)
def _layernorm(x, axis=-1, eps=1e-5):
    m = x.mean(dim=axis, keepdim=True)
    v = x.var(dim=axis, keepdim=True, unbiased=False)
    return (x - m) / torch.sqrt(v + eps)
def _scatter_add(tgt, idx, src, axis=0):
    return tgt.clone().index_add_(axis, idx, src)
def _segment_sum(src, seg, n):
    out = torch.zeros((n,) + src.shape[1:], dtype=src.dtype)
    return out.index_add_(0, seg, src)
def _pad(x, pw):
    flat = tuple(v for pair in reversed(pw) for v in pair)
    return torch.nn.functional.pad(x, flat)

kernel_name = 'gated_mix__spectral_fnet__sliding_local_dilated__prompt'

def setup_inputs():

    _w = (lambda a: (lambda t: t.to(torch.bfloat16) if t.is_floating_point() else t)(torch.as_tensor(a)))
    def _A_setup():
        B, S, D = 1, 2048, 640
        ns = torch.arange(S)[:, None] * torch.arange(S)[None, :]
        nd = torch.arange(D)[:, None] * torch.arange(D)[None, :]
        Ws_re = torch.cos(-2.0 * torch.pi * ns / S).to(float)
        Ws_im = torch.sin(-2.0 * torch.pi * ns / S).to(float)
        Wd_re = torch.cos(-2.0 * torch.pi * nd / D).to(float)
        Wd_im = torch.sin(-2.0 * torch.pi * nd / D).to(float)
        return {
            "x": _w((lambda *s: torch.randn(*s, dtype=torch.bfloat16) * 0.05)(B, S, D)),
            "dft_seq_re": _w(Ws_re),
            "dft_seq_im": _w(Ws_im),
            "dft_hid_re": _w(Wd_re),
            "dft_hid_im": _w(Wd_im),
        }
    _A_in = _A_setup()

    def _B_setup():
        B, S, D = 1, 2048, 640
        return {
            "x": _w((lambda *s: torch.randn(*s, dtype=torch.bfloat16) * 0.05)(B, S, D)),
            "wqkv": _w((lambda *s: torch.randn(*s, dtype=torch.bfloat16) * 1.6)(D, 3 * D)),
            "wo": _w((lambda *s: torch.randn(*s, dtype=torch.bfloat16) * 0.2)(D, D)),
        }
    _B_in = _B_setup()

    return {
        "A_x": _A_in["x"],
        "A_dft_seq_re": _A_in["dft_seq_re"],
        "A_dft_seq_im": _A_in["dft_seq_im"],
        "A_dft_hid_re": _A_in["dft_hid_re"],
        "A_dft_hid_im": _A_in["dft_hid_im"],
        "B_x": _B_in["x"],
        "B_wqkv": _B_in["wqkv"],
        "B_wo": _B_in["wo"],
        "gate_w": _w((lambda *s: torch.randn(*s, dtype=torch.bfloat16) * 0.05)(640) * 16.0),
    }

def reference(A_x, A_dft_seq_re, A_dft_seq_im, A_dft_hid_re, A_dft_hid_im, B_x, B_wqkv, B_wo, gate_w):
    def _A_ref(x, dft_seq_re, dft_seq_im, dft_hid_re, dft_hid_im):
        a = torch.einsum("bsd,ed->bse", x, dft_hid_re)
        b = torch.einsum("bsd,ed->bse", x, dft_hid_im)
        y = torch.einsum("ts,bse->bte", dft_seq_re, a) - torch.einsum("ts,bse->bte", dft_seq_im, b)
        return y

    def _B_ref(x, wqkv, wo):
        B, S, D = x.shape
        H = 32; Dh = D // H
        W, dil = 8, 2
        qkv = (lambda x, *d: x.permute(*d))((x @ wqkv).reshape(B, S, 3, H, Dh), 2, 0, 3, 1, 4)
        q, k, v = qkv[0], qkv[1], qkv[2]
        scale = Dh ** -0.5
        pos = torch.arange(S)
        off = torch.arange(W) * dil
        idx = pos[:, None] - off[None, :]
        mask = idx >= 0
        idx_c = torch.where(mask, idx, 0).reshape(-1)
        kg = k[:, :, idx_c, :].reshape(B, H, S, W, Dh)
        vg = v[:, :, idx_c, :].reshape(B, H, S, W, Dh)
        logits = torch.einsum("bhsd,bhswd->bhsw", q, kg) * scale
        logits = torch.where(mask[None, None, :, :], logits, -1e9)
        attn = _softmax(logits, axis=-1)
        o = torch.einsum("bhsw,bhswd->bhsd", attn, vg)
        return (lambda x, *d: x.permute(*d))(o, 0, 2, 1, 3).reshape(B, S, D) @ wo

    x = A_x
    g = _sigmoid((x * gate_w).sum(-1, keepdims=True))
    a = _A_ref(x, A_dft_seq_re, A_dft_seq_im, A_dft_hid_re, A_dft_hid_im)
    b = _B_ref(x, B_wqkv, B_wo)
    return g * a + (1 - g) * b
```

```cpp
#define PQROT 16
#include <hip/hip_runtime.h>
#include <hip/hip_bf16.h>
#include <type_traits>

typedef unsigned short u16;
typedef unsigned u32x4 __attribute__((ext_vector_type(4)));
typedef unsigned u32x2 __attribute__((ext_vector_type(2)));
typedef float f32x16 __attribute__((ext_vector_type(16)));
typedef float f32x4 __attribute__((ext_vector_type(4)));
typedef float f32x2 __attribute__((ext_vector_type(2)));
typedef __bf16 bf16x2 __attribute__((ext_vector_type(2)));
typedef __bf16 bf16x8 __attribute__((ext_vector_type(8)));
typedef __attribute__((address_space(3))) void* ldsp;
#define DI __device__ __forceinline__

#define AUXC 0
#define AUXW 0
#define DIAG 0
#define K2ROT 0
#define M16 1

constexpr int S = 2048, D = 640;
constexpr int NKS = 60;
constexpr size_t A2_BYTES = (size_t)S * 1920 * 2, W2_BYTES = (size_t)D * 1920 * 2;

DI unsigned cvtpk(float lo, float hi) { f32x2 v = {lo, hi}; bf16x2 b = __builtin_convertvector(v, bf16x2); return __builtin_bit_cast(unsigned, b); }
DI float dot2(unsigned a, unsigned b, float c) { return __builtin_amdgcn_fdot2_f32_bf16(__builtin_bit_cast(bf16x2, a), __builtin_bit_cast(bf16x2, b), c, false); }
#define BAR() asm volatile("s_barrier" ::: "memory")
template <int N> DI void wvm() { asm volatile("s_waitcnt vmcnt(%0)" ::"i"(N) : "memory"); }
template <int N> DI void wlg() { asm volatile("s_waitcnt lgkmcnt(%0)" ::"i"(N)); }
template <int OFF> DI u32x2 trrd(unsigned a) { u32x2 r; asm volatile("ds_read_b64_tr_b16 %0, %1 offset:%2" : "=v"(r) : "v"(a), "i"(OFF)); return r; }
template <int OFF> DI u32x2 rd64(unsigned a) { u32x2 r; asm volatile("ds_read_b64 %0, %1 offset:%2" : "=v"(r) : "v"(a), "i"(OFF)); return r; }
template <int OFF> DI u32x4 rd128(unsigned a) { u32x4 r; asm volatile("ds_read_b128 %0, %1 offset:%2" : "=v"(r) : "v"(a), "i"(OFF)); return r; }
DI u32x4 cat(u32x2 a, u32x2 b) { return __builtin_shufflevector(a, b, 0, 1, 2, 3); }
DI void mf32(f32x16& c, u32x4 a, u32x4 b) { asm volatile("v_mfma_f32_32x32x16_bf16 %0, %1, %2, %0" : "+v"(c) : "v"(a), "v"(b)); }
DI void mf16(f32x4& c, u32x4 a, u32x4 b) { asm volatile("v_mfma_f32_16x16x32_bf16 %0, %1, %2, %0" : "+v"(c) : "v"(a), "v"(b)); }
DI void vadd(unsigned& v, unsigned s) { asm volatile("v_add_u32 %0, %1, %0" : "+v"(v) : "s"(s)); }
template <int AUX> DI void dma(__amdgpu_buffer_rsrc_t rs, char* lds, unsigned voff, unsigned soff) {
  __builtin_amdgcn_raw_ptr_buffer_load_lds(rs, (ldsp)lds, 16, voff, soff, 0, AUX);
}
DI __amdgpu_buffer_rsrc_t mkrs(const void* p, unsigned bytes) { return __builtin_amdgcn_make_buffer_rsrc((void*)p, 0, bytes, 0x00020000); }

#define STAMP(i) do {} while (0)

DI int pq_off(int q) { return 4 * q; }
constexpr int PQ_HI = 16;
DI int o_col(int G, int j) { const int hg = G / 10, c = G % 10; return 80 * hg + (c < 8 ? 40 * (c >> 2) + 20 * (j >> 2) + 4 * (c & 3) + (j & 3) : 40 * (c - 8) + 20 * (j >> 2) + 16 + (j & 3)); }

constexpr int PNB = 4;
struct Frag { u32x2 al[2], ah[2]; u32x4 b[2]; };
struct Frag16 { u32x2 al[4], ah[4]; u32x4 b[4]; };

DI void pq_role(char* smem, const u16* x, const u16* Cr, const u16* Ci, const u16* Hr, const u16* Hi, const u16* Wo, u16* A2, u16* W2, int xcd, int j, int wid, int lane
) {
  const int mt = j / 5, nt = j % 5;
  const int t0 = xcd * 256 + (mt & 1) * 128, d0 = nt * 128;
  const u16* C = (mt >> 1) ? Ci : Cr;
  constexpr int NST = S / 64;
  if (wid >= 4) {
    const int lw = wid - 4;
    unsigned voff[8], sstep, ldsoff;
    __amdgpu_buffer_rsrc_t rs;
    if (lw < 2) {
      rs = mkrs(C + (size_t)t0 * S, 128 * S * 2);
#pragma unroll
      for (int i = 0; i < 8; ++i) {
        const int row = lw * 64 + 8 * i + (lane >> 3), t = (row & 96) + 2 * (row & 15) + ((row >> 4) & 1);
        voff[i] = t * (S * 2) + ((lane & 7) ^ ((row >> 1) & 7)) * 16;
      }
      sstep = 128; ldsoff = lw * 8192;
    } else {
      rs = mkrs(x + d0, S * D * 2 - d0 * 2);
#pragma unroll
      for (int i = 0; i < 8; ++i) { int row = (lw - 2) * 32 + 4 * i + (lane >> 4); voff[i] = row * (D * 2) + ((lane & 15) ^ (((row & 3) << 2) | ((row >> 2) & 3))) * 16; }
      sstep = 64 * D * 2; ldsoff = 16384 + (lw - 2) * 8192;
    }
    constexpr int P = PQROT ? PQROT : 32;
    const int rot = PQROT ? ((nt * P + 2) / 5 + mt * (P / 4) + xcd * (P / 8)) : 0, prot = xcd * 4;
    auto issue = [&](int st) {
      const unsigned so = ((((st & ~(P - 1)) + prot) & 31 & ~(P - 1)) + ((st + rot + (PQROT ? 0 : prot)) & (P - 1))) * sstep;
      char* dst = smem + (st & (PNB - 1)) * 32768 + ldsoff;
      if (lw < 2) {
#pragma unroll
        for (int i = 0; i < 8; ++i) dma<AUXC>(rs, dst + i * 1024, voff[i], so);
      } else {
#pragma unroll
        for (int i = 0; i < 8; ++i) dma<0>(rs, dst + i * 1024, voff[i], so);
      }
    };
    issue(0); issue(1);
    wvm<8>(); BAR();
    issue(2);
    for (int st = 0; st < NST - 3; ++st) { wvm<8>(); BAR(); issue(st + 3); }
    wvm<8>(); BAR();
    wvm<0>(); BAR();
    BAR();
  } else {
    u32x4 w[5]; u16* wdst;
    {
      const int rr = lane & 15, q = lane >> 4, e0 = 80 * xcd + 20 * (rr >> 2) + (rr & 3);
      int ks;
      if (wid < 2) {
        ks = 2 * j + wid;
        const u16* H = (ks < 20 ? Hr : Hi) + (ks % 20) * 32 + pq_off(q) + e0 * D;
#pragma unroll
        for (int i = 0; i < 5; ++i) w[i] = cat(*(const u32x2*)(H + 4 * i * D), *(const u32x2*)(H + 4 * i * D + PQ_HI));
      } else {
        ks = 40 + j;
#pragma unroll
        for (int jj = 0; jj < 4; ++jj) {
          const u16* src = Wo + o_col(4 * j + q, 4 * (wid - 2) + jj) * D + e0;
#pragma unroll
          for (int i = 0; i < 5; ++i) w[i][jj] = src[4 * i];
        }
      }
      wdst = W2 + (unsigned)(((xcd * NKS + ks) * 5 * 64 + lane) * 8);
    }
    auto w2store = [&]() {
      asm volatile("" : "+v"(w[0]), "+v"(w[1]), "+v"(w[2]), "+v"(w[3]), "+v"(w[4]));
      if (wid < 2) {
#pragma unroll
        for (int i = 0; i < 5; ++i) *(u32x4*)(wdst + i * 512) = w[i];
      } else {
#pragma unroll
        for (int i = 0; i < 5; ++i) *(u32x2*)(wdst + i * 512 + 4 * (wid - 2)) = u32x2{(w[i][0] & 0xffffu) | (w[i][1] << 16), (w[i][2] & 0xffffu) | (w[i][3] << 16)};
      }
    };
    const int wd = wid & 1, wt = wid >> 1;
    const unsigned sb = (unsigned)(size_t)(ldsp)smem;
    const int kbase = (mt >> 1) * 20 + nt * 4 + wd * 2;
    const unsigned sg = (mt >> 1) ? 0x80008000u : 0u;
    const int g = lane >> 4, q = (lane & 15) >> 2, p = lane & 3, r = lane & 15;
    unsigned xa[4][2], ca[2];
#pragma unroll
    for (int c = 0; c < 4; ++c)
#pragma unroll
      for (int t = 0; t < 2; ++t) { const int row = 8 * g + 4 * t + q; xa[c][t] = sb + 16384 + 256 * row + 16 * ((8 * wd + 2 * c + (p >> 1)) ^ (((row & 3) << 2) | ((row >> 2) & 3))) + 8 * (p & 1); }
#pragma unroll
    for (int ks = 0; ks < 2; ++ks) ca[ks] = sb + 128 * (64 * wt + r) + 16 * ((4 * ks + g) ^ ((r >> 1) & 7));
    f32x4 acc[4][4] = {};
#pragma unroll
    for (int c = 0; c < 4; ++c) asm volatile("" : "+v"(acc[c][0]), "+v"(acc[c][1]), "+v"(acc[c][2]), "+v"(acc[c][3]));
    Frag16 f0, f1;
#define PQ_G16(F, G, KS, E0, E1, E2, E3) do { \
      const u32x4 a0 = cat(F.al[0], F.ah[0]), a1 = cat(F.al[1], F.ah[1]), a2 = cat(F.al[2], F.ah[2]), a3 = cat(F.al[3], F.ah[3]); \
      wlg<6>(); \
      mf16(acc[0][0], a0, F.b[0]); G.b[0] = rd128<0>(ca[KS]); \
      mf16(acc[0][1], a0, F.b[1]); G.al[0] = trrd<8192 * KS>(xa[0][0]); \
      mf16(acc[0][2], a0, F.b[2]); G.ah[0] = trrd<8192 * KS>(xa[0][1]); \
      mf16(acc[0][3], a0, F.b[3]); G.b[1] = rd128<2048>(ca[KS]); \
      wlg<8>(); \
      mf16(acc[1][3], a1, F.b[3]); G.b[2] = rd128<4096>(ca[KS]); \
      mf16(acc[1][2], a1, F.b[2]); G.b[3] = rd128<6144>(ca[KS]); \
      mf16(acc[1][1], a1, F.b[1]); G.al[1] = trrd<8192 * KS>(xa[1][0]); \
      mf16(acc[1][0], a1, F.b[0]); G.ah[1] = trrd<8192 * KS>(xa[1][1]); \
      wlg<10>(); \
      mf16(acc[2][0], a2, F.b[0]); G.al[2] = trrd<8192 * KS>(xa[2][0]); \
      mf16(acc[2][1], a2, F.b[1]); G.ah[2] = trrd<8192 * KS>(xa[2][1]); \
      mf16(acc[2][2], a2, F.b[2]); G.al[3] = trrd<8192 * KS>(xa[3][0]); \
      mf16(acc[2][3], a2, F.b[3]); G.ah[3] = trrd<8192 * KS>(xa[3][1]); \
      wlg<12>(); \
      mf16(acc[3][3], a3, F.b[3]); E0; \
      mf16(acc[3][2], a3, F.b[2]); E1; \
      mf16(acc[3][1], a3, F.b[1]); E2; \
      mf16(acc[3][0], a3, F.b[0]); E3; } while (0)
    BAR();
    if (wid == 0) STAMP(1);
    f0.b[0] = rd128<0>(ca[0]); f0.al[0] = trrd<0>(xa[0][0]); f0.ah[0] = trrd<0>(xa[0][1]);
    f0.b[1] = rd128<2048>(ca[0]); f0.b[2] = rd128<4096>(ca[0]); f0.b[3] = rd128<6144>(ca[0]);
#pragma unroll
    for (int c = 1; c < 4; ++c) { f0.al[c] = trrd<0>(xa[c][0]); f0.ah[c] = trrd<0>(xa[c][1]); }
    vadd(ca[0], 32768);
#define PQ_STAGE16 do { \
      const unsigned dl = ((st & 3) == 3) ? (unsigned)(-3 * 32768) : 32768u, dl2 = ((st & 3) == 2) ? (unsigned)(-3 * 32768) : 32768u; \
      PQ_G16(f0, f1, 1, { vadd(xa[0][0], dl); vadd(xa[0][1], dl); }, { vadd(xa[1][0], dl); vadd(xa[1][1], dl); }, { vadd(xa[2][0], dl); vadd(xa[2][1], dl); }, { vadd(xa[3][0], dl); vadd(xa[3][1], dl); }); \
      BAR(); \
      PQ_G16(f1, f0, 0, { vadd(ca[1], dl); }, { vadd(ca[0], dl2); }, {}, {}); } while (0)
#pragma unroll 1
    for (int st = 0; st < 3; ++st) PQ_STAGE16;
    w2store();
#pragma unroll 1
    for (int st = 3; st < NST; ++st) PQ_STAGE16;
    asm volatile("s_waitcnt lgkmcnt(0)\n\ts_nop 15" : "+v"(f0.b[0]), "+v"(f0.b[1]), "+v"(f0.b[2]), "+v"(f0.b[3]), "+v"(f0.al[0]), "+v"(f0.al[1]), "+v"(f0.al[2]), "+v"(f0.al[3]),
                 "+v"(f0.ah[0]), "+v"(f0.ah[1]), "+v"(f0.ah[2]), "+v"(f0.ah[3]));
#pragma unroll
    for (int c = 0; c < 4; ++c) asm volatile("" : "+v"(acc[c][0]), "+v"(acc[c][1]), "+v"(acc[c][2]), "+v"(acc[c][3]));
    if (wid == 0) STAMP(2);
#pragma unroll
    for (int c = 0; c < 2; ++c)
#pragma unroll
      for (int tt = 0; tt < 4; ++tt) {
        const u32x4 v = {cvtpk(acc[2 * c][tt][0], acc[2 * c][tt][1]) ^ sg, cvtpk(acc[2 * c][tt][2], acc[2 * c][tt][3]) ^ sg,
                         cvtpk(acc[2 * c + 1][tt][0], acc[2 * c + 1][tt][1]) ^ sg, cvtpk(acc[2 * c + 1][tt][2], acc[2 * c + 1][tt][3]) ^ sg};
        *(u32x4*)(A2 + (unsigned)(((((t0 >> 4) + 4 * wt + tt) * NKS + kbase + c) * 64 + lane) * 8)) = v;
      }
  }
}

constexpr int XST = 24576, WUN = 15360, WBASE = 3 * XST;
constexpr int qkv_wait(int s) { return s == 10 ? 8 : s == 11 ? 10 : s == 12 ? 12 : s == 13 ? 12 : s == 14 ? 10 : 6; }

struct QS { f32x4 acc[15][3]; u32x2 wl[5], wh[5]; u32x2 xl[2][3], xh[2][3]; unsigned wa, wb, xa[2][2]; };

template <int F, int HI> DI u32x2 qkv_wrd(const QS& z) {
  if constexpr (F % 5 < 4) return trrd<160 * (F / 5) + 40 * (F % 5) + 7680 * HI>(z.wa);
  else return trrd<160 * (F / 5) + 7680 * HI>(z.wb);
}
template <int PAR, int SL, int T> DI void qkv_mm(QS& z) {
  const u32x4 w = cat(z.wl[SL % 5], z.wh[SL % 5]), xx = cat(z.xl[PAR][T], z.xh[PAR][T]);
  if constexpr (SL < 10) mf16(z.acc[SL][T], w, xx);
  else mf16(z.acc[SL][T], xx, w);
}
template <int PAR, int SL> DI void qkv_slot(QS& z, unsigned dw, unsigned dx) {
  constexpr int F = (SL + 4) % 15, NP = PAR ^ 1;
  wlg<qkv_wait(SL)>();
  qkv_mm<PAR, SL, 0>(z);
  z.wl[F % 5] = qkv_wrd<F, 0>(z);
  qkv_mm<PAR, SL, 1>(z);
  z.wh[F % 5] = qkv_wrd<F, 1>(z);
  qkv_mm<PAR, SL, 2>(z);
  if constexpr (SL >= 9 && SL <= 11) { z.xl[NP][SL - 9] = rd64<2048 * (SL - 9)>(z.xa[NP][0]); z.xh[NP][SL - 9] = rd64<2048 * (SL - 9)>(z.xa[NP][1]); }
  if constexpr (SL == 8) BAR();
  if constexpr (SL == 10) vadd(z.wa, dw);
  if constexpr (SL == 13) vadd(z.wb, dw);
  if constexpr (SL == 12) { vadd(z.xa[NP][0], dx); vadd(z.xa[NP][1], dx); }
}
template <int PAR> DI void qkv_unit(QS& z, unsigned dw, unsigned dx) {
  qkv_slot<PAR, 0>(z, dw, dx); qkv_slot<PAR, 1>(z, dw, dx); qkv_slot<PAR, 2>(z, dw, dx); qkv_slot<PAR, 3>(z, dw, dx); qkv_slot<PAR, 4>(z, dw, dx);
  qkv_slot<PAR, 5>(z, dw, dx); qkv_slot<PAR, 6>(z, dw, dx); qkv_slot<PAR, 7>(z, dw, dx); qkv_slot<PAR, 8>(z, dw, dx); qkv_slot<PAR, 9>(z, dw, dx);
  qkv_slot<PAR, 10>(z, dw, dx); qkv_slot<PAR, 11>(z, dw, dx); qkv_slot<PAR, 12>(z, dw, dx); qkv_slot<PAR, 13>(z, dw, dx); qkv_slot<PAR, 14>(z, dw, dx);
}
DI f32x4 mfb(u32x4 a, u32x4 b, f32x4 c) { return __builtin_amdgcn_mfma_f32_16x16x32_bf16(__builtin_bit_cast(bf16x8, a), __builtin_bit_cast(bf16x8, b), c, 0, 0, 0); }
DI u32x2 pk4(f32x4 v) { return u32x2{cvtpk(v[0], v[1]), cvtpk(v[2], v[3])}; }
DI float fmx(float a, float b) { return __builtin_elementwise_maximum(a, b); }
DI float fmx3(float a, float b, float c) { return fmx(fmx(a, b), c); }
DI void swap32(float& a, float& b) { auto r = __builtin_amdgcn_permlane32_swap(__float_as_uint(a), __float_as_uint(b), false, false); a = __uint_as_float(r[0]); b = __uint_as_float(r[1]); }
DI void swap16(float& a, float& b) { auto r = __builtin_amdgcn_permlane16_swap(__float_as_uint(a), __float_as_uint(b), false, false); a = __uint_as_float(r[0]); b = __uint_as_float(r[1]); }

typedef unsigned u32x8 __attribute__((ext_vector_type(8)));
template <int T> DI u32x4 win(u32x8 v) { return __builtin_shufflevector(v, v, 2 * T, 2 * T + 1, 2 * T + 2, 2 * T + 3); }

DI void attn_tile(const u32x4 (&qf)[4], const u32x4 (&kp)[4], const u32x4 (&kc)[4], const u32x4 (&v)[4], const u32x4 (&v4)[4], f32x4 bp, f32x4 bc, u32x2 mp,
                  int g, u32x4& oa, u32x4& ob, u32x2& oc) {
  constexpr float SC = 0.22360679774997896f * 1.4426950408889634f;
  f32x4 sv[4]; float mx[4];
#pragma unroll
  for (int h = 0; h < 4; ++h) {
    const f32x4 sp = mfb(kp[h], qf[h], bp), sc = mfb(kc[h], qf[h], bc);
#pragma unroll
    for (int e = 0; e < 4; ++e) sv[h][e] = fmx(sp[e], sc[e]);
    mx[h] = fmx(fmx3(sv[h][0], sv[h][1], sv[h][2]), sv[h][3]);
  }
  swap32(mx[0], mx[2]); swap32(mx[1], mx[3]);
  float ma = fmx(mx[0], mx[2]), mb = fmx(mx[1], mx[3]);
  swap16(ma, mb);
  float m0 = fmx(ma, mb) * -SC, m1 = m0;
  swap16(m0, m1);
  float m2 = m0, m3 = m1;
  swap32(m0, m2); swap32(m1, m3);
  mx[0] = m0; mx[1] = m1; mx[2] = m2; mx[3] = m3;
  const u32x4 ones = {0x3f803f80u, 0x3f803f80u, 0x3f803f80u, 0x3f803f80u};
  const f32x4 zero = {0.f, 0.f, 0.f, 0.f};
  f32x4 o[4], o4 = zero; float inv[4];
#pragma unroll
  for (int h = 0; h < 4; ++h) {
    f32x4 p;
#pragma unroll
    for (int e = 0; e < 4; ++e) p[e] = __builtin_amdgcn_exp2f(__builtin_fmaf(sv[h][e], SC, mx[h]));
    const u32x2 pk = pk4(p);
    const u32x4 pf = {pk[0] & mp[0], pk[1] & mp[1], pk[0] & ~mp[0], pk[1] & ~mp[1]};
    inv[h] = __builtin_amdgcn_rcpf(mfb(ones, pf, zero)[0]);
    o[h] = mfb(v[h], pf, zero);
    o4 = mfb(v4[h], pf, o4);
  }
  oa = cat(pk4(o[0] * inv[0]), pk4(o[1] * inv[1]));
  ob = cat(pk4(o[2] * inv[2]), pk4(o[3] * inv[3]));
  const float ia = (g & 1) ? inv[1] : inv[0], ib = (g & 1) ? inv[3] : inv[2];
  oc = pk4(o4 * ((g & 2) ? ib : ia));
}

DI void qkv_role(char* smem, const u16* x, const u16* Wqkv, const u16* gw, u16* A2, float* gate, int hg, int m, int wid, int lane
) {
  const int rb = 176 * m - 16;
  const int rot = m % 10;
  if (wid >= 4) {
    const int lw = wid - 4;
    if (lw < 2) {
      const __amdgpu_buffer_rsrc_t rs = mkrs(Wqkv, D * 1920 * 2);
      unsigned voff[8];
#pragma unroll
      for (int i = 0; i < 8; ++i) { const int n = (lw * 8 + i) * 64 + lane, row = n / 30, c = n % 30; voff[i] = row * 3840 + (c / 10) * 1280 + hg * 160 + (c % 10) * 16; }
      auto issue = [&](int u, int buf) {
        int X = (u >> 1) + rot; X = X >= 10 ? X - 10 : X;
        const unsigned so = (2 * X + (u & 1)) * (32 * 3840);
        char* dst = smem + WBASE + buf * WUN + lw * 8192;
#pragma unroll
        for (int i = 0; i < 7; ++i) dma<AUXW>(rs, dst + i * 1024, voff[i], so);
        if (lw == 0) dma<AUXW>(rs, dst + 7 * 1024, voff[7], so);
      };
      issue(0, 0); issue(1, 1);
      if (lw == 0) wvm<8>(); else wvm<7>();
      BAR();
      issue(2, 2); issue(3, 3);
      int buf = 4;
      for (int u = 0; u < 16; ++u) {
        if (lw == 0) wvm<16>(); else wvm<14>();
        BAR();
        issue(u + 4, buf);
        buf = (buf == 4) ? 0 : buf + 1;
      }
      if (lw == 0) wvm<16>(); else wvm<14>();
      BAR();
      if (lw == 0) wvm<8>(); else wvm<7>();
      BAR();
      wvm<0>(); BAR();
    } else {
      const __amdgpu_buffer_rsrc_t rs = mkrs(x, S * D * 2);
      unsigned voff[12];
#pragma unroll
      for (int i = 0; i < 12; ++i) {
        const int u = (lw - 2) * 48 + (i % 6) * 8 + (lane >> 3);
        const int tk = min(max(rb + 2 * u + i / 6, 0), S - 1);
        voff[i] = tk * (D * 2) + ((lane & 7) ^ ((u >> 1) & 7)) * 16;
      }
      auto issue2 = [&](int Xs, int buf, int half) {
        int X = Xs + rot; X = X >= 10 ? X - 10 : X;
        char* dst = smem + buf * XST + (lw - 2) * 6144;
        if (half == 0) {
#pragma unroll
          for (int i = 0; i < 6; ++i) dma<0>(rs, dst + i * 1024, voff[i], X * 128);
        } else {
#pragma unroll
          for (int i = 0; i < 6; ++i) dma<0>(rs, dst + 12288 + i * 1024, voff[6 + i], X * 128);
        }
      };
      issue2(0, 0, 0); issue2(0, 0, 1);
      wvm<0>(); BAR();
      issue2(1, 1, 0); issue2(1, 1, 1);
      int buf = 2;
      for (int X = 2; X < 10; ++X) {
        BAR();
        issue2(X, buf, 0);
        wvm<6>(); BAR();
        issue2(X, buf, 1);
        buf = (buf == 2) ? 0 : buf + 1;
      }
      BAR();
      wvm<0>(); BAR();
      BAR();
    }
    BAR();
    BAR();
    const int rr = lane & 15, q = lane >> 4;
    u32x4 gv[5], xv[2][5];
#pragma unroll
    for (int c = 0; c < 5; ++c) gv[c] = *(const u32x4*)(gw + (c * 16 + rr) * 8);
#pragma unroll
    for (int it = 0; it < 2; ++it)
#pragma unroll
      for (int c = 0; c < 5; ++c) xv[it][c] = *(const u32x4*)(x + min(176 * m + 22 * hg + 8 * lw + 4 * it + q, S - 1) * D + (c * 16 + rr) * 8);
#pragma unroll
    for (int it = 0; it < 2; ++it) {
      float z = 0.f;
#pragma unroll
      for (int c = 0; c < 5; ++c)
#pragma unroll
        for (int e = 0; e < 4; ++e) z = dot2(xv[it][c][e], gv[c][e], z);
      z += __shfl_xor(z, 1); z += __shfl_xor(z, 2); z += __shfl_xor(z, 4); z += __shfl_xor(z, 8);
      const int i = 8 * lw + 4 * it + q, row = 176 * m + 22 * hg + i;
      if (rr == 0 && i < 22 && row < S) gate[row] = z;
    }
  } else {
    const unsigned sb = (unsigned)(size_t)(ldsp)smem;
    const int g = lane >> 4, r = lane & 15;
    QS z;
#pragma unroll
    for (int f = 0; f < 15; ++f) {
#pragma unroll
      for (int tt = 0; tt < 3; ++tt) z.acc[f][tt] = f32x4{0.f, 0.f, 0.f, 0.f};
      asm volatile("" : "+v"(z.acc[f][0]), "+v"(z.acc[f][1]), "+v"(z.acc[f][2]));
    }
    z.wa = sb + WBASE + (4 * g + (r >> 2)) * 480 + 8 * (r & 3);
    z.wb = sb + WBASE + (4 * g + (r >> 2)) * 480 + 40 * (r & 3) + 32;
#pragma unroll
    for (int ks = 0; ks < 2; ++ks)
#pragma unroll
      for (int t = 0; t < 2; ++t) z.xa[ks][t] = sb + (48 * wid + r) * 128 + 16 * ((4 * ks + 2 * t + (g >> 1)) ^ ((r >> 1) & 7)) + 8 * (g & 1);
    BAR();
    if (wid == 0) STAMP(1);
    z.wl[0] = qkv_wrd<0, 0>(z); z.wh[0] = qkv_wrd<0, 1>(z);
    z.xl[0][0] = rd64<0>(z.xa[0][0]); z.xh[0][0] = rd64<0>(z.xa[0][1]);
    z.xl[0][1] = rd64<2048>(z.xa[0][0]); z.xh[0][1] = rd64<2048>(z.xa[0][1]);
    z.xl[0][2] = rd64<4096>(z.xa[0][0]); z.xh[0][2] = rd64<4096>(z.xa[0][1]);
    z.wl[1] = qkv_wrd<1, 0>(z); z.wh[1] = qkv_wrd<1, 1>(z);
    z.wl[2] = qkv_wrd<2, 0>(z); z.wh[2] = qkv_wrd<2, 1>(z);
    z.wl[3] = qkv_wrd<3, 0>(z); z.wh[3] = qkv_wrd<3, 1>(z);
    vadd(z.xa[0][0], XST); vadd(z.xa[0][1], XST);
    int wb = 0, xb = 0;
    for (int X = 0; X < 10; ++X) {
      const unsigned dw0 = (wb == 4) ? (unsigned)(-4 * WUN) : (unsigned)WUN; wb = (wb == 4) ? 0 : wb + 1;
      const unsigned dw1 = (wb == 4) ? (unsigned)(-4 * WUN) : (unsigned)WUN; wb = (wb == 4) ? 0 : wb + 1;
      const unsigned dx0 = (xb == 2) ? (unsigned)(-2 * XST) : (unsigned)XST; xb = (xb == 2) ? 0 : xb + 1;
      const unsigned dx1 = (xb == 2) ? (unsigned)(-2 * XST) : (unsigned)XST;
      qkv_unit<0>(z, dw0, dx0);
      qkv_unit<1>(z, dw1, dx1);
    }
    asm volatile("s_waitcnt lgkmcnt(0)\n\ts_nop 15" : "+v"(z.wl[0]), "+v"(z.wl[1]), "+v"(z.wl[2]), "+v"(z.wl[3]), "+v"(z.wh[0]), "+v"(z.wh[1]), "+v"(z.wh[2]), "+v"(z.wh[3]),
                 "+v"(z.xl[0][0]), "+v"(z.xl[0][1]), "+v"(z.xl[0][2]), "+v"(z.xh[0][0]), "+v"(z.xh[0][1]), "+v"(z.xh[0][2]));
#pragma unroll
    for (int f = 0; f < 15; ++f) asm volatile("" : "+v"(z.acc[f][0]), "+v"(z.acc[f][1]), "+v"(z.acc[f][2]));
    if (wid == 0) STAMP(2);
    const int par = wid >> 1, hf = wid & 1;
    u32x4 kf[4][4];
    u32x8 vf[5], v4[4];
    char* xch = smem + par * 5120 + lane * 16;
    if (hf == 0) {
#pragma unroll
      for (int i = 0; i < 5; ++i) *(u32x4*)(xch + i * 1024) = cat(pk4(z.acc[5 + i][2]), pk4(z.acc[10 + i][2]));
    }
    asm volatile("s_waitcnt lgkmcnt(0)\n\ts_barrier" ::: "memory");
    if (wid == 0) STAMP(3);
    {
      u32x4 t[5];
#pragma unroll
      for (int i = 0; i < 5; ++i) t[i] = *(const u32x4*)(xch + i * 1024);
#pragma unroll
      for (int h = 0; h < 4; ++h) kf[0][h] = u32x4{t[h][0], t[h][1], t[4][0], t[4][1]};
#pragma unroll
      for (int i = 0; i < 5; ++i) {
        const u32x2 a = pk4(z.acc[10 + i][0]), b = pk4(z.acc[10 + i][1]), c = pk4(z.acc[10 + i][2]);
        vf[i] = u32x8{t[i][2], t[i][3], a[0], a[1], b[0], b[1], c[0], c[1]};
      }
    }
#pragma unroll
    for (int h = 0; h < 4; ++h) {
      const bool mine = (r >> 2) == h;
#pragma unroll
      for (int e = 0; e < 8; ++e) v4[h][e] = mine ? vf[4][e] : 0u;
    }
    constexpr float NEG = -1e30f;
    f32x4 bp, bc, bp0, bc0; u32x2 mp;
#pragma unroll
    for (int e = 0; e < 4; ++e) {
      const int kk = 4 * g + e;
      bp[e] = kk >= r + 9 ? 0.f : NEG;
      bc[e] = (kk <= r && kk + 7 >= r) ? 0.f : NEG;
      bp0[e] = hf ? bp[e] : NEG;
      bc0[e] = (hf == 0 && m == 0 && kk < 8) ? NEG : bc[e];
    }
    mp[0] = (4 * g >= r + 9 ? 0xffffu : 0u) | (4 * g + 1 >= r + 9 ? 0xffff0000u : 0u);
    mp[1] = (4 * g + 2 >= r + 9 ? 0xffffu : 0u) | (4 * g + 3 >= r + 9 ? 0xffff0000u : 0u);
    const __amdgpu_buffer_rsrc_t ro = mkrs(A2 + 1280 * hg, (unsigned)A2_BYTES - 2560 * hg);
    auto tile = [&](auto TT) {
      constexpr int tt = decltype(TT)::value;
      u32x4 qf[4], vw[4], v4w[4];
      const u32x2 q4 = pk4(z.acc[4][tt]), k4 = pk4(z.acc[9][tt]);
#pragma unroll
      for (int h = 0; h < 4; ++h) {
        qf[h] = cat(pk4(z.acc[h][tt]), g == h ? q4 : u32x2{0u, 0u});
        kf[tt + 1][h] = cat(pk4(z.acc[5 + h][tt]), k4);
        vw[h] = win<tt>(vf[h]); v4w[h] = win<tt>(v4[h]);
      }
      u32x4 oa, ob; u32x2 oc;
      attn_tile(qf, kf[tt], kf[tt + 1], vw, v4w, tt == 0 ? bp0 : bp, tt == 0 ? bc0 : bc, mp, g, oa, ob, oc);
      const int u = 48 * hf + 16 * tt + r, tok = rb + 2 * u + par;
      const unsigned base = (u >= 8 && tok < S) ? (unsigned)(((2 * (tok >> 5) + par) * NKS + 40) * 1024 + ((tok >> 1) & 15) * 16) : 0x80000000u;
      __builtin_amdgcn_raw_buffer_store_b128(oa, ro, base + g * 256, 0, 0);
      __builtin_amdgcn_raw_buffer_store_b128(ob, ro, base + g * 256 + 1024, 0, 0);
      __builtin_amdgcn_raw_buffer_store_b64(oc, ro, base + (g >> 1) * 256 + (g & 1) * 8 + 2048, 0, 0);
    };
    tile(std::integral_constant<int, 0>{}); tile(std::integral_constant<int, 1>{}); tile(std::integral_constant<int, 2>{});
  }
}

__global__ __launch_bounds__(512, 2) void k1(const u16* __restrict__ x, const u16* __restrict__ Cr, const u16* __restrict__ Ci, const u16* __restrict__ Hr,
                                             const u16* __restrict__ Hi, const u16* __restrict__ Wqkv, const u16* __restrict__ Wo, const u16* __restrict__ gw,
                                             u16* __restrict__ A2, u16* __restrict__ W2, float* __restrict__ gate
) {
  __shared__ __attribute__((aligned(16))) char smem[163840];
  const int wid = __builtin_amdgcn_readfirstlane(threadIdx.x >> 6), lane = threadIdx.x & 63;
  const int xcd = blockIdx.x & 7, j = blockIdx.x >> 3;
  if (wid == 0) STAMP(0);
  if (j < 20) pq_role(smem, x, Cr, Ci, Hr, Hi, Wo, A2, W2, xcd, j, wid, lane);
  else qkv_role(smem, x, Wqkv, gw, A2, gate, xcd, j - 20, wid, lane);
}

__global__ __launch_bounds__(256, 1) void k2(const u16* __restrict__ A2, const u16* __restrict__ W2, const float* __restrict__ gate, u16* __restrict__ out
) {
  __shared__ __attribute__((aligned(16))) char red[4 * 3 * 5 * 1024];
  const int wid = __builtin_amdgcn_readfirstlane(threadIdx.x >> 6), lane = threadIdx.x & 63;
  const int xcd = blockIdx.x & 7, j = blockIdx.x >> 3, mt = xcd * 4 + (j >> 3), n8 = j & 7;
  if (wid == 0) STAMP(8);
  const __amdgpu_buffer_rsrc_t ra = mkrs(A2 + (unsigned)(mt * 4 * NKS * 512), 4 * NKS * 1024), rw = mkrs(W2 + (unsigned)(n8 * NKS * 5 * 512), NKS * 5 * 1024);
  f32x4 acc[5][4] = {};
#define DEP 3
  u32x4 fa[DEP][4], fw[DEP][5];
  const int ro = K2ROT ? (j >> 3) + ((j >> 4) & (j >> 3) & 1) : 0, rp = K2ROT ? ((j >> 3) * 5 + 1) >> 1 : 0;
  auto load = [&](int s) {
    int ks;
    if (s < 5) { ks = s + ro; ks = 40 + wid * 5 + (ks >= 5 ? ks - 5 : ks); } else { ks = s - 5 + rp; ks = wid * 10 + (ks >= 10 ? ks - 10 : ks); }
#pragma unroll
    for (int i = 0; i < 5; ++i) fw[s % DEP][i] = __builtin_amdgcn_raw_buffer_load_b128(rw, lane * 16 + (i & 3) * 1024, ks * 5120 + (i >> 2) * 4096, 0);
#pragma unroll
    for (int t = 0; t < 4; ++t) fa[s % DEP][t] = __builtin_amdgcn_raw_buffer_load_b128(ra, lane * 16, (((t + wid) & 3) * NKS + ks) * 1024, 0);
  };
#pragma unroll
  for (int s = 0; s < DEP; ++s) load(s);
  float zt[4];
#pragma unroll
  for (int t = 0; t < 4; ++t) { const int tg = (t + wid) & 3; zt[t] = gate[mt * 64 + 32 * (tg >> 1) + 2 * (lane & 15) + (tg & 1)]; }
  __builtin_amdgcn_sched_barrier(0);
  float gt[4];
#pragma unroll
  for (int s = 0; s < 15; ++s) {
    if (s == 5) {
#pragma unroll
      for (int t = 0; t < 4; ++t) {
        const float ez = __expf(-fmaxf(zt[t], -60.f));
        gt[t] = 1.f / (1.f + ez);
#pragma unroll
        for (int i = 0; i < 5; ++i) acc[i][t] *= ez;
      }
    }
#pragma unroll
    for (int i = 0; i < 5; ++i)
#pragma unroll
      for (int t = 0; t < 4; ++t) acc[i][t] = mfb(fw[s % DEP][i], fa[s % DEP][t], acc[i][t]);
    __builtin_amdgcn_sched_barrier(0);
    if (s + DEP < 15) load(s + DEP);
    __builtin_amdgcn_sched_barrier(0);
  }
  if (wid == 0) STAMP(9);
  f32x4 own[5];
#pragma unroll
  for (int t = 1; t < 4; ++t)
#pragma unroll
    for (int i = 0; i < 5; ++i) *(f32x4*)(red + ((((t + wid) & 3) * 3 + t - 1) * 5 + i) * 1024 + lane * 16) = gt[t] * acc[i][t];
#pragma unroll
  for (int i = 0; i < 5; ++i) own[i] = gt[0] * acc[i][0];
  __syncthreads();
#pragma unroll
  for (int p = 0; p < 3; ++p)
#pragma unroll
    for (int i = 0; i < 5; ++i) own[i] += *(const f32x4*)(red + ((wid * 3 + p) * 5 + i) * 1024 + lane * 16);
  u16* dst = out + (unsigned)((mt * 64 + 32 * (wid >> 1) + 2 * (lane & 15) + (wid & 1)) * D + n8 * 80 + 20 * (lane >> 4));
  typedef unsigned u32x4u __attribute__((ext_vector_type(4), aligned(8)));
  *(u32x4u*)dst = u32x4{cvtpk(own[0][0], own[0][1]), cvtpk(own[0][2], own[0][3]), cvtpk(own[1][0], own[1][1]), cvtpk(own[1][2], own[1][3])};
  *(u32x4u*)(dst + 8) = u32x4{cvtpk(own[2][0], own[2][1]), cvtpk(own[2][2], own[2][3]), cvtpk(own[3][0], own[3][1]), cvtpk(own[3][2], own[3][3])};
  *(u32x2*)(dst + 16) = u32x2{cvtpk(own[4][0], own[4][1]), cvtpk(own[4][2], own[4][3])};
}

extern "C" void kernel_launch(void* const* d_in, const int* in_sizes, int n_in, void* d_out, int out_size, void* d_ws, size_t ws_size, hipStream_t stream) {
  const u16* x = (const u16*)d_in[0];
  u16* A2 = (u16*)d_ws;
  u16* W2 = (u16*)((char*)d_ws + A2_BYTES);
  float* gate = (float*)((char*)d_ws + A2_BYTES + W2_BYTES);
  k1<<<256, 512, 0, stream>>>(x, (const u16*)d_in[1], (const u16*)d_in[2], (const u16*)d_in[3], (const u16*)d_in[4], (const u16*)d_in[6], (const u16*)d_in[7], (const u16*)d_in[8], A2, W2, gate);
  k2<<<256, 256, 0, stream>>>(A2, W2, gate, (u16*)d_out);
}
```
